```python
import torch

def _softmax(x, axis=-1):
    return torch.softmax(x, dim=axis)
def _gelu(x):
    return torch.nn.functional.gelu(x, approximate="tanh")
def _silu(x):
    return torch.nn.functional.silu(x)
def _sigmoid(x):
    return torch.sigmoid(x)
def _layernorm(x, axis=-1, eps=1e-5):
    m = x.mean(dim=axis, keepdim=True)
    v = x.var(dim=axis, keepdim=True, unbiased=False)
    return (x - m) / torch.sqrt(v + eps)
def _scatter_add(tgt, idx, src, axis=0):
    return tgt.clone().index_add_(axis, idx, src)
def _segment_sum(src, seg, n):
    out = torch.zeros((n,) + src.shape[1:], dtype=src.dtype)
    return out.index_add_(0, seg, src)
def _pad(x, pw):
    flat = tuple(v for pair in reversed(pw) for v in pair)
    return torch.nn.functional.pad(x, flat)

kernel_name = 'standalone__layernorm_linear__sampling'

def setup_inputs():

    _w = (lambda a: (lambda t: t.to(torch.bfloat16) if t.is_floating_point() else t)(torch.as_tensor(a)))
    B, S, D = 1024, 8, 640
    return {
        "x": _w((lambda *s: torch.randn(*s, dtype=torch.bfloat16) * 0.05)(B, S, D)),
        "g": _w((lambda *s: torch.randn(*s, dtype=torch.bfloat16) * 0.05)(D) + 1.0),
        "b": _w((lambda *s: torch.randn(*s, dtype=torch.bfloat16) * 0.4)(D)),
        "w": _w((lambda *s: torch.randn(*s, dtype=torch.bfloat16) * 0.05)(D, D)),
    }

def reference(x, g, b, w):
    return (_layernorm(x, axis=-1) * g + b) @ w
```

```cpp
#include <hip/hip_runtime.h>
#include <hip/hip_bf16.h>
#include <stdint.h>

typedef __attribute__((ext_vector_type(8))) short bf16x8;
typedef __attribute__((ext_vector_type(4))) short s16x4;
typedef __attribute__((ext_vector_type(16))) float f32x16;
typedef __attribute__((ext_vector_type(4))) float f32x4;
typedef __attribute__((ext_vector_type(4))) unsigned u32x4;
typedef __attribute__((ext_vector_type(2))) unsigned u32x2;
typedef __attribute__((ext_vector_type(2))) __bf16 bf16x2_t;
typedef unsigned short u16;

constexpr int D = 640;
constexpr int BM = 128, BN = 160, KC = 64, NCH = D / KC;
constexpr int WBUF = KC * BN * 2;
constexpr int NBUF = 3;
constexpr int GB_OFF = NBUF * WBUF;
constexpr int SMEM = GB_OFF + 2 * D * 4;

__device__ __forceinline__ float lo_f(unsigned u) { return __builtin_bit_cast(float, u << 16); }
__device__ __forceinline__ float hi_f(unsigned u) { return __builtin_bit_cast(float, u & 0xffff0000u); }
__device__ __forceinline__ unsigned pk_bf16(float a, float b) {
    bf16x2_t v; v[0] = (__bf16)a; v[1] = (__bf16)b;
    return __builtin_bit_cast(unsigned, v);
}
__device__ __forceinline__ s16x4 tr_read(const char* p) {
    return __builtin_bit_cast(s16x4, __builtin_amdgcn_ds_read_tr16_b64_v4i16(
        (__attribute__((address_space(3))) s16x4*)p));
}

__global__ __launch_bounds__(256, 1) void ln_linear_kernel(
    const u16* __restrict__ x, const u16* __restrict__ g, const u16* __restrict__ b,
    const u16* __restrict__ w, u16* __restrict__ out)
{
    __shared__ __attribute__((aligned(16))) char smem[SMEM];
    const int tid = threadIdx.x, lane = tid & 63, wid = tid >> 6;
    const int bid = blockIdx.x;
    const int xcd = bid & 7, idx = bid >> 3;
    const int tn = idx & 3, tm = xcd * 8 + (idx >> 2);
    const int row0 = tm * BM + wid * 32;
    const int col0 = tn * BN;
    const int r = lane & 31, h = lane >> 5;

    u32x4 xa[40];
    {
        const u32x4* xp = reinterpret_cast<const u32x4*>(x + (size_t)(row0 + r) * D + h * 8);
#pragma unroll
        for (int s = 0; s < 40; ++s) xa[s] = xp[2 * s];
    }

    const char* wsrc[5];
#pragma unroll
    for (int i = 0; i < 5; ++i) {
        int o = (wid * 5 + i) * 1024 + lane * 16;
        int k = o / 320, cb = o % 320;
        wsrc[i] = reinterpret_cast<const char*>(w) + (size_t)k * (D * 2) + col0 * 2 + cb;
    }
    auto stage_w = [&](int c) {
        char* dst = smem + (c % NBUF) * WBUF + wid * 5 * 1024;
#pragma unroll
        for (int i = 0; i < 5; ++i)
            __builtin_amdgcn_global_load_lds(
                (const __attribute__((address_space(1))) void*)(wsrc[i] + (size_t)c * KC * D * 2),
                (__attribute__((address_space(3))) void*)(dst + i * 1024), 16, 0, 0);
    };
    stage_w(0);
    stage_w(1);

    float* gsm = reinterpret_cast<float*>(smem + GB_OFF);
    float* bsm = gsm + D;
    for (int i = tid; i < D; i += 256) {
        gsm[i] = __builtin_bit_cast(float, (unsigned)g[i] << 16);
        bsm[i] = __builtin_bit_cast(float, (unsigned)b[i] << 16);
    }

    float s1a = 0.f, s1b = 0.f, s2a = 0.f, s2b = 0.f;
    const bf16x2_t ones = __builtin_bit_cast(bf16x2_t, 0x3f803f80u);
#pragma unroll
    for (int s = 0; s < 40; ++s) {
#pragma unroll
        for (int i = 0; i < 4; i += 2) {
            const unsigned u0 = xa[s][i], u1 = xa[s][i + 1];
            bf16x2_t v0 = __builtin_bit_cast(bf16x2_t, u0);
            bf16x2_t v1 = __builtin_bit_cast(bf16x2_t, u1);
            s1a = __builtin_amdgcn_fdot2_f32_bf16(v0, ones, s1a, false);
            s2a = __builtin_amdgcn_fdot2_f32_bf16(v0, v0, s2a, false);
            s1b = __builtin_amdgcn_fdot2_f32_bf16(v1, ones, s1b, false);
            s2b = __builtin_amdgcn_fdot2_f32_bf16(v1, v1, s2b, false);
        }
    }
    float s1 = s1a + s1b, s2 = s2a + s2b;
    s1 += __shfl_xor(s1, 32);
    s2 += __shfl_xor(s2, 32);
    const float mean = s1 * (1.0f / D);
    float var = s2 * (1.0f / D) - mean * mean;
    var = var < 0.f ? 0.f : var;
    const float rstd = 1.0f / sqrtf(var + 1e-5f);
    const float nm = -mean * rstd;

    f32x16 acc[5];
#pragma unroll
    for (int t = 0; t < 5; ++t)
#pragma unroll
        for (int i = 0; i < 16; ++i) acc[t][i] = 0.f;

    const int G = lane >> 4, li = lane & 15;
    const int trbase = (8 * (G >> 1) + (li >> 2)) * 320 + (16 * (G & 1) + 4 * (li & 3)) * 2;

#pragma unroll
    for (int c = 0; c < NCH; ++c) {
        if (c < NCH - 1) asm volatile("s_waitcnt vmcnt(5)" ::: "memory");
        else             asm volatile("s_waitcnt vmcnt(0)" ::: "memory");
        asm volatile("s_waitcnt lgkmcnt(0)" ::: "memory");
        __builtin_amdgcn_s_barrier();
        if (c + 2 < NCH) stage_w(c + 2);
        const char* wb = smem + (c % NBUF) * WBUF + trbase;
#pragma unroll
        for (int ks = 0; ks < 4; ++ks) {
            const int s = c * 4 + ks;
            const f32x4 g0 = *reinterpret_cast<const f32x4*>(gsm + 16 * s + 8 * h);
            const f32x4 g1 = *reinterpret_cast<const f32x4*>(gsm + 16 * s + 8 * h + 4);
            const f32x4 b0 = *reinterpret_cast<const f32x4*>(bsm + 16 * s + 8 * h);
            const f32x4 b1 = *reinterpret_cast<const f32x4*>(bsm + 16 * s + 8 * h + 4);
            u32x4 af;
            af[0] = pk_bf16(fmaf(fmaf(lo_f(xa[s][0]), rstd, nm), g0[0], b0[0]),
                            fmaf(fmaf(hi_f(xa[s][0]), rstd, nm), g0[1], b0[1]));
            af[1] = pk_bf16(fmaf(fmaf(lo_f(xa[s][1]), rstd, nm), g0[2], b0[2]),
                            fmaf(fmaf(hi_f(xa[s][1]), rstd, nm), g0[3], b0[3]));
            af[2] = pk_bf16(fmaf(fmaf(lo_f(xa[s][2]), rstd, nm), g1[0], b1[0]),
                            fmaf(fmaf(hi_f(xa[s][2]), rstd, nm), g1[1], b1[1]));
            af[3] = pk_bf16(fmaf(fmaf(lo_f(xa[s][3]), rstd, nm), g1[2], b1[2]),
                            fmaf(fmaf(hi_f(xa[s][3]), rstd, nm), g1[3], b1[3]));
            const bf16x8 xf = __builtin_bit_cast(bf16x8, af);
#pragma unroll
            for (int t = 0; t < 5; ++t) {
                s16x4 w0 = tr_read(wb + ks * 16 * 320 + t * 64);
                s16x4 w1 = tr_read(wb + ks * 16 * 320 + t * 64 + 1280);
                bf16x8 wf = __builtin_shufflevector(w0, w1, 0, 1, 2, 3, 4, 5, 6, 7);
                acc[t] = __builtin_amdgcn_mfma_f32_32x32x16_bf16(wf, xf, acc[t], 0, 0, 0);
            }
        }
    }

    u16* orow = out + (size_t)(row0 + r) * D + col0 + 4 * h;
#pragma unroll
    for (int t = 0; t < 5; ++t)
#pragma unroll
        for (int q = 0; q < 4; ++q) {
            u32x2 v;
            v[0] = pk_bf16(acc[t][4 * q + 0], acc[t][4 * q + 1]);
            v[1] = pk_bf16(acc[t][4 * q + 2], acc[t][4 * q + 3]);
            *reinterpret_cast<u32x2*>(orow + 32 * t + 8 * q) = v;
        }
}

extern "C" void kernel_launch(void* const* d_in, const int* in_sizes, int n_in,
                              void* d_out, int out_size, void* d_ws, size_t ws_size,
                              hipStream_t stream) {
    const u16* x = (const u16*)d_in[0];
    const u16* g = (const u16*)d_in[1];
    const u16* b = (const u16*)d_in[2];
    const u16* w = (const u16*)d_in[3];
    u16* out = (u16*)d_out;
    ln_linear_kernel<<<256, 256, 0, stream>>>(x, g, b, w, out);
}
```

```cpp
#include <hip/hip_runtime.h>
#include <hip/hip_bf16.h>
#include <stdint.h>

typedef __attribute__((ext_vector_type(8))) short bf16x8;
typedef __attribute__((ext_vector_type(4))) short s16x4;
typedef __attribute__((ext_vector_type(4))) float f32x4;
typedef __attribute__((ext_vector_type(4))) unsigned u32x4;
typedef __attribute__((ext_vector_type(2))) unsigned u32x2;
typedef __attribute__((ext_vector_type(2))) __bf16 bf16x2_t;
typedef unsigned short u16;
typedef __attribute__((address_space(3))) char lds_char;

constexpr int D = 640;
constexpr int BM = 128, BN = 160, KC = 64, NCH = D / KC;
constexpr int WPITCH = 352;
constexpr int WBUF = KC * WPITCH;
constexpr int NWB = 4;
constexpr int ABUF = BM * KC * 2;
constexpr int NAB = 4;
constexpr int A_OFF = NWB * WBUF;
constexpr int GB_OFF = A_OFF + NAB * ABUF;
constexpr int SMEM_TOTAL = GB_OFF + 2 * D * 4;
constexpr int NWAVE = 12;

__device__ __forceinline__ float lo_f(unsigned u) { return __builtin_bit_cast(float, u << 16); }
__device__ __forceinline__ float hi_f(unsigned u) { return __builtin_bit_cast(float, u & 0xffff0000u); }
__device__ __forceinline__ unsigned pk_bf16(float a, float b) {
    bf16x2_t v; v[0] = (__bf16)a; v[1] = (__bf16)b;
    return __builtin_bit_cast(unsigned, v);
}
__device__ __forceinline__ float fma1(float a, float b, float c) {
    float r; asm("v_fma_f32 %0, %1, %2, %3" : "=v"(r) : "v"(a), "v"(b), "v"(c)); return r;
}
__device__ __forceinline__ s16x4 tr_read(const char* p) {
    return __builtin_bit_cast(s16x4, __builtin_amdgcn_ds_read_tr16_b64_v4i16(
        (__attribute__((address_space(3))) s16x4*)p));
}
__device__ __forceinline__ unsigned lds_addr(const void* p) {
    return (unsigned)(uintptr_t)(lds_char*)p;
}
__device__ __forceinline__ void glds16(const void* sbase, unsigned voff, unsigned ldst) {
    asm volatile("s_mov_b32 m0, %2\n\ts_nop 0\n\tglobal_load_lds_dwordx4 %0, %1"
                 :: "v"(voff), "s"(sbase), "s"(ldst) : "memory");
}

#define STAMP(i)
#define TNOW(v)

__global__ __launch_bounds__(NWAVE * 64, 3) void ln_linear_kernel(
    const u16* __restrict__ x, const u16* __restrict__ g, const u16* __restrict__ b,
    const u16* __restrict__ w, u16* __restrict__ out
    )
{
    __shared__ __attribute__((aligned(16))) char smem[SMEM_TOTAL];
    const int tid = threadIdx.x, lane = tid & 63;
    const int wid = __builtin_amdgcn_readfirstlane(tid >> 6);
    const int bid = blockIdx.x;
    const int xcd = bid & 7, idx = bid >> 3;
    const int tn = idx & 3, tm = xcd * 8 + (idx >> 2);
    const int col0 = tn * BN;
    float* gsm = reinterpret_cast<float*>(smem + GB_OFF);
    float* bsm = gsm + D;
    STAMP(0);
    unsigned long long bsum = 0, vsum = 0;

    if (wid >= 4) {
        const int hw = wid - 4;
        const int lr = lane >> 3, lc = lane & 7;
        const int rbase = hw * 16;
        u32x4 xv[2][NCH];
        {
            const char* xp = reinterpret_cast<const char*>(x) + (size_t)(tm * BM + rbase + lr) * 1280 + lc * 16;
#pragma unroll
            for (int c = 0; c < NCH; ++c)
#pragma unroll
                for (int i = 0; i < 2; ++i)
                    xv[i][c] = *reinterpret_cast<const u32x4*>(xp + i * 8 * 1280 + c * 128);
        }
        const unsigned smem_base = lds_addr(smem);
        unsigned wvoff[3];
#pragma unroll
        for (int i = 0; i < 3; ++i) {
            int pc = hw + 8 * i;
            int o = pc * 1024 + lane * 16;
            int pos = o / WPITCH, sl = (o % WPITCH) >> 4;
            pos = pos < KC ? pos : 0;
            sl = sl < 20 ? sl : 0;
            int k = (pos & ~12) | ((pos & 4) << 1) | ((pos & 8) >> 1);
            wvoff[i] = k * (D * 2) + sl * 16;
        }
        const char* wbase = reinterpret_cast<const char*>(w) + col0 * 2;
        auto stage_w = [&](int c) {
#pragma unroll
            for (int i = 0; i < 3; ++i)
                if (i < 2 || hw < 6)
                    glds16(wbase + (size_t)c * KC * D * 2, wvoff[i],
                           smem_base + (c & (NWB - 1)) * WBUF + (hw + 8 * i) * 1024);
        };
        stage_w(0); stage_w(1); stage_w(2);
        {
            const int t = tid - 256;
            if (t < 160) {
                const u32x2 gv = *reinterpret_cast<const u32x2*>(g + t * 4);
                const u32x2 bv = *reinterpret_cast<const u32x2*>(b + t * 4);
                f32x4 gf = {lo_f(gv[0]), hi_f(gv[0]), lo_f(gv[1]), hi_f(gv[1])};
                f32x4 bf = {lo_f(bv[0]), hi_f(bv[0]), lo_f(bv[1]), hi_f(bv[1])};
                *reinterpret_cast<f32x4*>(gsm + t * 4) = gf;
                *reinterpret_cast<f32x4*>(bsm + t * 4) = bf;
            }
        }
        STAMP(1);
        float rs[2], nm[2];
        {
            const bf16x2_t ones = __builtin_bit_cast(bf16x2_t, 0x3f803f80u);
            float s1[2][2] = {{0.f, 0.f}, {0.f, 0.f}}, s2[2][2] = {{0.f, 0.f}, {0.f, 0.f}};
#pragma unroll
            for (int c = 0; c < NCH; ++c)
#pragma unroll
                for (int j = 0; j < 4; ++j)
#pragma unroll
                    for (int i = 0; i < 2; ++i) {
                        const unsigned u = xv[i][c][j];
                        bf16x2_t v = __builtin_bit_cast(bf16x2_t, u);
                        s1[i][j & 1] = __builtin_amdgcn_fdot2_f32_bf16(v, ones, s1[i][j & 1], false);
                        s2[i][j & 1] = __builtin_amdgcn_fdot2_f32_bf16(v, v, s2[i][j & 1], false);
                    }
#pragma unroll
            for (int i = 0; i < 2; ++i) {
                float a1 = s1[i][0] + s1[i][1], a2 = s2[i][0] + s2[i][1];
                a1 += __shfl_xor(a1, 1); a2 += __shfl_xor(a2, 1);
                a1 += __shfl_xor(a1, 2); a2 += __shfl_xor(a2, 2);
                a1 += __shfl_xor(a1, 4); a2 += __shfl_xor(a2, 4);
                const float mean = a1 * (1.0f / D);
                float var = a2 * (1.0f / D) - mean * mean;
                var = var < 0.f ? 0.f : var;
                rs[i] = __builtin_amdgcn_rsqf(var + 1e-5f);
                nm[i] = -mean * rs[i];
            }
        }
        asm volatile("s_waitcnt lgkmcnt(0)" ::: "memory");
        __builtin_amdgcn_s_barrier();
        STAMP(2);
        const int awr = A_OFF + (rbase + lr) * 128;
        auto norm_chunk = [&](int c) {
            const f32x4 g0 = *reinterpret_cast<const f32x4*>(gsm + c * 64 + lc * 8);
            const f32x4 g1 = *reinterpret_cast<const f32x4*>(gsm + c * 64 + lc * 8 + 4);
            const f32x4 b0 = *reinterpret_cast<const f32x4*>(bsm + c * 64 + lc * 8);
            const f32x4 b1 = *reinterpret_cast<const f32x4*>(bsm + c * 64 + lc * 8 + 4);
            float f[2][8];
#pragma unroll
            for (int i = 0; i < 2; ++i)
#pragma unroll
                for (int j = 0; j < 4; ++j) {
                    const unsigned u = xv[i][c][j];
                    f[i][2 * j] = lo_f(u); f[i][2 * j + 1] = hi_f(u);
                }
#pragma unroll
            for (int i = 0; i < 2; ++i)
#pragma unroll
                for (int j = 0; j < 8; ++j) f[i][j] = fma1(f[i][j], rs[i], nm[i]);
#pragma unroll
            for (int i = 0; i < 2; ++i)
#pragma unroll
                for (int j = 0; j < 4; ++j) {
                    f[i][j] = fma1(f[i][j], g0[j], b0[j]);
                    f[i][j + 4] = fma1(f[i][j + 4], g1[j], b1[j]);
                }
#pragma unroll
            for (int i = 0; i < 2; ++i) {
                u32x4 o;
#pragma unroll
                for (int j = 0; j < 4; ++j) o[j] = pk_bf16(f[i][2 * j], f[i][2 * j + 1]);
                const int rr = rbase + i * 8 + lr;
                *reinterpret_cast<u32x4*>(smem + awr + (c & (NAB - 1)) * ABUF + i * 1024 + ((lc ^ ((rr >> 1) & 7)) << 4)) = o;
            }
        };
        norm_chunk(0);
        norm_chunk(1);
        STAMP(3);
#pragma unroll
        for (int c = 0; c < NCH; ++c) {
            unsigned long long ta = 0, tb = 0, tc = 0;
            TNOW(ta);
            if (c + 2 < NCH) {
                if (hw < 6) asm volatile("s_waitcnt vmcnt(3)" ::: "memory");
                else        asm volatile("s_waitcnt vmcnt(2)" ::: "memory");
            } else          asm volatile("s_waitcnt vmcnt(0)" ::: "memory");
            asm volatile("s_waitcnt lgkmcnt(0)" ::: "memory");
            TNOW(tb);
            __builtin_amdgcn_s_barrier();
            TNOW(tc);
            if (c > 0) { vsum += tb - ta; bsum += tc - tb; }
            if (c + 3 < NCH) stage_w(c + 3);
            if (c + 2 < NCH) norm_chunk(c + 2);
        }
        STAMP(4);
        STAMP(5);
        return;
    }

    const int wm = wid >> 1, wn = wid & 1;
    const int l15 = lane & 15, lq = lane >> 4;
    STAMP(1);
    __builtin_amdgcn_s_barrier();

    f32x4 acc[4][5];
#pragma unroll
    for (int m = 0; m < 4; ++m)
#pragma unroll
        for (int t = 0; t < 5; ++t) acc[m][t] = (f32x4){0.f, 0.f, 0.f, 0.f};

    const int q4 = l15 >> 2, p4 = l15 & 3;
    const int trbase = (16 * (lq >> 1) + 4 * (lq & 1) + q4) * WPITCH + (wn * 80 + 4 * p4) * 2;
    unsigned ard[2];
#pragma unroll
    for (int ks = 0; ks < 2; ++ks)
        ard[ks] = A_OFF + (wm * 64 + l15) * 128 + (((4 * ks + lq) ^ ((l15 >> 1) & 7)) << 4);

    auto ld_w = [&](const char* wb, int ks, bf16x8 (&wf)[5]) {
#pragma unroll
        for (int t = 0; t < 5; ++t) {
            s16x4 w0 = tr_read(wb + ks * 32 * WPITCH + t * 32);
            s16x4 w1 = tr_read(wb + ks * 32 * WPITCH + t * 32 + 8 * WPITCH);
            wf[t] = __builtin_shufflevector(w0, w1, 0, 1, 2, 3, 4, 5, 6, 7);
        }
    };
    auto ld_a = [&](int ab, int ks, bf16x8 (&xf)[4]) {
#pragma unroll
        for (int m = 0; m < 4; ++m)
            xf[m] = *reinterpret_cast<const bf16x8*>(smem + ab + ard[ks] + m * 2048);
    };

    auto ld_all = [&](const char* wb, int ab, int ks, bf16x8 (&wf)[5], bf16x8 (&xf)[4]) {
        xf[0] = *reinterpret_cast<const bf16x8*>(smem + ab + ard[ks]);
        ld_w(wb, ks, wf);
#pragma unroll
        for (int m = 1; m < 4; ++m)
            xf[m] = *reinterpret_cast<const bf16x8*>(smem + ab + ard[ks] + m * 2048);
    };
    bf16x8 wfA[5], wfB[5], xfA[4], xfB[4];
    __builtin_amdgcn_s_barrier();
    STAMP(2);
    __builtin_amdgcn_s_setprio(1);
    ld_a(0, 0, xfA);
    ld_w(smem + trbase, 0, wfA);

#define MFMA20(WF, XF) \
    _Pragma("unroll") for (int m = 0; m < 4; ++m) \
    _Pragma("unroll") for (int t = 0; t < 5; ++t) \
        acc[m][t] = __builtin_amdgcn_mfma_f32_16x16x32_bf16(WF[t], XF[m], acc[m][t], 0, 0, 0); \
    _Pragma("unroll") for (int t = 0; t < 7; ++t) { \
        __builtin_amdgcn_sched_group_barrier(0x8, 1, 0); \
        __builtin_amdgcn_sched_group_barrier(0x100, 2, 0); } \
    __builtin_amdgcn_sched_group_barrier(0x8, 13, 0); \
    __builtin_amdgcn_sched_barrier(0);

#pragma unroll 1
    for (int c = 0; c < NCH; ++c) {
        const char* wb = smem + (c & (NWB - 1)) * WBUF + trbase;
        const char* wbn = smem + ((c + 1) & (NWB - 1)) * WBUF + trbase;
        const int ab = (c & (NAB - 1)) * ABUF, abn = ((c + 1) & (NAB - 1)) * ABUF;
        __builtin_amdgcn_sched_barrier(0);
        ld_all(wb, ab, 1, wfB, xfB);
        MFMA20(wfA, xfA);
        ld_all(wbn, abn, 0, wfA, xfA);
        MFMA20(wfB, xfB);
        if (c + 1 < NCH) {
            unsigned long long tb = 0, tc = 0;
            asm volatile("s_waitcnt lgkmcnt(0)" ::: "memory");
            TNOW(tb);
            __builtin_amdgcn_s_barrier();
            TNOW(tc);
            bsum += tc - tb;
        }
    }
    STAMP(3);

    {
        constexpr int OP = 176;
        char* ot = smem + wid * (64 * OP);
        asm volatile("s_waitcnt lgkmcnt(0)" ::: "memory");
        __builtin_amdgcn_s_barrier();
#pragma unroll
        for (int m = 0; m < 4; ++m)
#pragma unroll
            for (int t = 0; t < 5; ++t) {
                u32x2 v;
                v[0] = pk_bf16(acc[m][t][0], acc[m][t][1]);
                v[1] = pk_bf16(acc[m][t][2], acc[m][t][3]);
                *reinterpret_cast<u32x2*>(ot + (16 * m + l15) * OP + (16 * t + 4 * lq) * 2) = v;
            }
        char* ob = reinterpret_cast<char*>(out) + (size_t)(tm * BM + wm * 64) * 1280 + (col0 + wn * 80) * 2;
#pragma unroll
        for (int i = 0; i < 10; ++i) {
            const int o = i * 64 + lane;
            const int rr = o / 10, cc = o % 10;
            const u32x4 v = *reinterpret_cast<const u32x4*>(ot + rr * OP + cc * 16);
            *reinterpret_cast<u32x4*>(ob + rr * 1280 + cc * 16) = v;
        }
    }
    STAMP(4);
    STAMP(5);
}

extern "C" void kernel_launch(void* const* d_in, const int* in_sizes, int n_in,
                              void* d_out, int out_size, void* d_ws, size_t ws_size,
                              hipStream_t stream) {
    const u16* x = (const u16*)d_in[0];
    const u16* g = (const u16*)d_in[1];
    const u16* b = (const u16*)d_in[2];
    const u16* w = (const u16*)d_in[3];
    u16* out = (u16*)d_out;
    ln_linear_kernel<<<256, NWAVE * 64, 0, stream>>>(x, g, b, w, out);
}
```

```cpp
#include <hip/hip_runtime.h>
#include <hip/hip_bf16.h>
#include <stdint.h>

typedef __attribute__((ext_vector_type(8))) short bf16x8;
typedef __attribute__((ext_vector_type(4))) short s16x4;
typedef __attribute__((ext_vector_type(4))) float f32x4;
typedef __attribute__((ext_vector_type(4))) unsigned u32x4;
typedef __attribute__((ext_vector_type(2))) unsigned u32x2;
typedef __attribute__((ext_vector_type(2))) __bf16 bf16x2_t;
typedef unsigned short u16;
typedef __attribute__((address_space(3))) char lds_char;

constexpr int D = 640;
constexpr int BM = 128, BN = 160, KC = 64, NCH = D / KC;
constexpr int WPITCH = 352;
constexpr int WBUF = KC * WPITCH;
constexpr int NWB = 4;
constexpr int ABUF = BM * KC * 2;
constexpr int NAB = 4;
constexpr int A_OFF = NWB * WBUF;
constexpr int GB_OFF = A_OFF + NAB * ABUF;
constexpr int SMEM_TOTAL = GB_OFF + 2 * D * 4;
constexpr int NWAVE = 12;

__device__ __forceinline__ float lo_f(unsigned u) { return __builtin_bit_cast(float, u << 16); }
__device__ __forceinline__ float hi_f(unsigned u) { return __builtin_bit_cast(float, u & 0xffff0000u); }
__device__ __forceinline__ unsigned pk_bf16(float a, float b) {
    bf16x2_t v; v[0] = (__bf16)a; v[1] = (__bf16)b;
    return __builtin_bit_cast(unsigned, v);
}
__device__ __forceinline__ float fma1(float a, float b, float c) {
    float r; asm("v_fma_f32 %0, %1, %2, %3" : "=v"(r) : "v"(a), "v"(b), "v"(c)); return r;
}
__device__ __forceinline__ s16x4 tr_read(const char* p) {
    return __builtin_bit_cast(s16x4, __builtin_amdgcn_ds_read_tr16_b64_v4i16(
        (__attribute__((address_space(3))) s16x4*)p));
}
__device__ __forceinline__ unsigned lds_addr(const void* p) {
    return (unsigned)(uintptr_t)(lds_char*)p;
}
__device__ __forceinline__ void glds16(const void* sbase, unsigned voff, unsigned ldst) {
    asm volatile("s_mov_b32 m0, %2\n\ts_nop 0\n\tglobal_load_lds_dwordx4 %0, %1"
                 :: "v"(voff), "s"(sbase), "s"(ldst) : "memory");
}

#define STAMP(i)
#define TNOW(v)

__global__ __launch_bounds__(NWAVE * 64, 3) void ln_linear_kernel(
    const u16* __restrict__ x, const u16* __restrict__ g, const u16* __restrict__ b,
    const u16* __restrict__ w, u16* __restrict__ out
    )
{
    __shared__ __attribute__((aligned(16))) char smem[SMEM_TOTAL];
    const int tid = threadIdx.x, lane = tid & 63;
    const int wid = __builtin_amdgcn_readfirstlane(tid >> 6);
    const int bid = blockIdx.x;
    const int xcd = bid & 7, idx = bid >> 3;
    const int tn = idx & 3, tm = xcd * 8 + (idx >> 2);
    const int col0 = tn * BN;
    float* gsm = reinterpret_cast<float*>(smem + GB_OFF);
    float* bsm = gsm + D;
    STAMP(0);
    unsigned long long bsum = 0, vsum = 0;

    if (wid >= 4) {
        const int hw = wid - 4;
        const int lr = lane >> 3, lc = lane & 7;
        const int rbase = hw * 16;
        u32x4 xv[2][NCH];
        {
            const char* xp = reinterpret_cast<const char*>(x) + (size_t)(tm * BM + rbase + lr) * 1280 + lc * 16;
#pragma unroll
            for (int c = 0; c < NCH; ++c)
#pragma unroll
                for (int i = 0; i < 2; ++i)
                    xv[i][c] = *reinterpret_cast<const u32x4*>(xp + i * 8 * 1280 + c * 128);
        }
        const unsigned smem_base = lds_addr(smem);
        unsigned wvoff[3];
#pragma unroll
        for (int i = 0; i < 3; ++i) {
            int pc = hw + 8 * i;
            int o = pc * 1024 + lane * 16;
            int pos = o / WPITCH, sl = (o % WPITCH) >> 4;
            pos = pos < KC ? pos : 0;
            sl = sl < 20 ? sl : 0;
            int k = (pos & ~12) | ((pos & 4) << 1) | ((pos & 8) >> 1);
            wvoff[i] = k * (D * 2) + sl * 16;
        }
        const char* wbase = reinterpret_cast<const char*>(w) + col0 * 2;
        auto stage_w = [&](int c) {
#pragma unroll
            for (int i = 0; i < 3; ++i)
                if (i < 2 || hw < 6)
                    glds16(wbase + (size_t)c * KC * D * 2, wvoff[i],
                           smem_base + (c & (NWB - 1)) * WBUF + (hw + 8 * i) * 1024);
        };
        stage_w(0); stage_w(1); stage_w(2);
        {
            const int t = tid - 256;
            if (t < 160) {
                const u32x2 gv = *reinterpret_cast<const u32x2*>(g + t * 4);
                const u32x2 bv = *reinterpret_cast<const u32x2*>(b + t * 4);
                f32x4 gf = {lo_f(gv[0]), hi_f(gv[0]), lo_f(gv[1]), hi_f(gv[1])};
                f32x4 bf = {lo_f(bv[0]), hi_f(bv[0]), lo_f(bv[1]), hi_f(bv[1])};
                *reinterpret_cast<f32x4*>(gsm + t * 4) = gf;
                *reinterpret_cast<f32x4*>(bsm + t * 4) = bf;
            }
        }
        STAMP(1);
        float rs[2], nm[2];
        {
            const bf16x2_t ones = __builtin_bit_cast(bf16x2_t, 0x3f803f80u);
            float s1[2][2] = {{0.f, 0.f}, {0.f, 0.f}}, s2[2][2] = {{0.f, 0.f}, {0.f, 0.f}};
#pragma unroll
            for (int c = 0; c < NCH; ++c)
#pragma unroll
                for (int j = 0; j < 4; ++j)
#pragma unroll
                    for (int i = 0; i < 2; ++i) {
                        const unsigned u = xv[i][c][j];
                        bf16x2_t v = __builtin_bit_cast(bf16x2_t, u);
                        s1[i][j & 1] = __builtin_amdgcn_fdot2_f32_bf16(v, ones, s1[i][j & 1], false);
                        s2[i][j & 1] = __builtin_amdgcn_fdot2_f32_bf16(v, v, s2[i][j & 1], false);
                    }
#pragma unroll
            for (int i = 0; i < 2; ++i) {
                float a1 = s1[i][0] + s1[i][1], a2 = s2[i][0] + s2[i][1];
                a1 += __shfl_xor(a1, 1); a2 += __shfl_xor(a2, 1);
                a1 += __shfl_xor(a1, 2); a2 += __shfl_xor(a2, 2);
                a1 += __shfl_xor(a1, 4); a2 += __shfl_xor(a2, 4);
                const float mean = a1 * (1.0f / D);
                float var = a2 * (1.0f / D) - mean * mean;
                var = var < 0.f ? 0.f : var;
                rs[i] = __builtin_amdgcn_rsqf(var + 1e-5f);
                nm[i] = -mean * rs[i];
            }
        }
        asm volatile("s_waitcnt lgkmcnt(0)" ::: "memory");
        __builtin_amdgcn_s_barrier();
        STAMP(2);
        const int awr = A_OFF + (rbase + lr) * 128;
        auto norm_chunk = [&](int c) {
            const f32x4 g0 = *reinterpret_cast<const f32x4*>(gsm + c * 64 + lc * 8);
            const f32x4 g1 = *reinterpret_cast<const f32x4*>(gsm + c * 64 + lc * 8 + 4);
            const f32x4 b0 = *reinterpret_cast<const f32x4*>(bsm + c * 64 + lc * 8);
            const f32x4 b1 = *reinterpret_cast<const f32x4*>(bsm + c * 64 + lc * 8 + 4);
            float f[2][8];
#pragma unroll
            for (int i = 0; i < 2; ++i)
#pragma unroll
                for (int j = 0; j < 4; ++j) {
                    const unsigned u = xv[i][c][j];
                    f[i][2 * j] = lo_f(u); f[i][2 * j + 1] = hi_f(u);
                }
#pragma unroll
            for (int i = 0; i < 2; ++i)
#pragma unroll
                for (int j = 0; j < 8; ++j) f[i][j] = fma1(f[i][j], rs[i], nm[i]);
#pragma unroll
            for (int i = 0; i < 2; ++i)
#pragma unroll
                for (int j = 0; j < 4; ++j) {
                    f[i][j] = fma1(f[i][j], g0[j], b0[j]);
                    f[i][j + 4] = fma1(f[i][j + 4], g1[j], b1[j]);
                }
#pragma unroll
            for (int i = 0; i < 2; ++i) {
                u32x4 o;
#pragma unroll
                for (int j = 0; j < 4; ++j) o[j] = pk_bf16(f[i][2 * j], f[i][2 * j + 1]);
                const int rr = rbase + i * 8 + lr;
                *reinterpret_cast<u32x4*>(smem + awr + (c & (NAB - 1)) * ABUF + i * 1024 + ((lc ^ ((rr >> 1) & 7)) << 4)) = o;
            }
        };
        norm_chunk(0);
        norm_chunk(1);
        STAMP(3);
#pragma unroll
        for (int c = 0; c < NCH; ++c) {
            unsigned long long ta = 0, tb = 0, tc = 0;
            TNOW(ta);
            if (c + 2 < NCH) {
                if (hw < 6) asm volatile("s_waitcnt vmcnt(3)" ::: "memory");
                else        asm volatile("s_waitcnt vmcnt(2)" ::: "memory");
            } else          asm volatile("s_waitcnt vmcnt(0)" ::: "memory");
            asm volatile("s_waitcnt lgkmcnt(0)" ::: "memory");
            TNOW(tb);
            __builtin_amdgcn_s_barrier();
            TNOW(tc);
            if (c > 0) { vsum += tb - ta; bsum += tc - tb; }
            if (c + 3 < NCH) stage_w(c + 3);
            if (c + 2 < NCH) norm_chunk(c + 2);
        }
        STAMP(4);
        STAMP(5);
        return;
    }

    const int wm = wid >> 1, wn = wid & 1;
    const int l15 = lane & 15, lq = lane >> 4;
    unsigned pf_sink = 0;
    {
        const char* wp = reinterpret_cast<const char*>(w) + col0 * 2;
#pragma unroll
        for (int i = 0; i < 6; ++i) {
            const int e = i * 256 + tid;
            const int row = 192 + e / 3, ln = e % 3;
            if (row < D) pf_sink ^= *reinterpret_cast<const unsigned*>(wp + row * 1280 + ln * 128);
        }
    }
    STAMP(1);
    __builtin_amdgcn_s_barrier();

    f32x4 acc[4][5];
#pragma unroll
    for (int m = 0; m < 4; ++m)
#pragma unroll
        for (int t = 0; t < 5; ++t) acc[m][t] = (f32x4){0.f, 0.f, 0.f, 0.f};

    const int q4 = l15 >> 2, p4 = l15 & 3;
    const int trbase = (16 * (lq >> 1) + 4 * (lq & 1) + q4) * WPITCH + (wn * 80 + 4 * p4) * 2;
    unsigned ard[2];
#pragma unroll
    for (int ks = 0; ks < 2; ++ks)
        ard[ks] = A_OFF + (wm * 64 + l15) * 128 + (((4 * ks + lq) ^ ((l15 >> 1) & 7)) << 4);

    auto ld_w = [&](const char* wb, int ks, bf16x8 (&wf)[5]) {
#pragma unroll
        for (int t = 0; t < 5; ++t) {
            s16x4 w0 = tr_read(wb + ks * 32 * WPITCH + t * 32);
            s16x4 w1 = tr_read(wb + ks * 32 * WPITCH + t * 32 + 8 * WPITCH);
            wf[t] = __builtin_shufflevector(w0, w1, 0, 1, 2, 3, 4, 5, 6, 7);
        }
    };
    auto ld_a = [&](int ab, int ks, bf16x8 (&xf)[4]) {
#pragma unroll
        for (int m = 0; m < 4; ++m)
            xf[m] = *reinterpret_cast<const bf16x8*>(smem + ab + ard[ks] + m * 2048);
    };

    auto ld_all = [&](const char* wb, int ab, int ks, bf16x8 (&wf)[5], bf16x8 (&xf)[4]) {
        xf[0] = *reinterpret_cast<const bf16x8*>(smem + ab + ard[ks]);
        ld_w(wb, ks, wf);
#pragma unroll
        for (int m = 1; m < 4; ++m)
            xf[m] = *reinterpret_cast<const bf16x8*>(smem + ab + ard[ks] + m * 2048);
    };
    bf16x8 wfA[5], wfB[5], xfA[4], xfB[4];
    asm volatile("" :: "v"(pf_sink));
    __builtin_amdgcn_s_barrier();
    STAMP(2);
    __builtin_amdgcn_s_setprio(1);
    ld_a(0, 0, xfA);
    ld_w(smem + trbase, 0, wfA);

#define MFMA20(WF, XF) \
    _Pragma("unroll") for (int m = 0; m < 4; ++m) \
    _Pragma("unroll") for (int t = 0; t < 5; ++t) \
        acc[m][t] = __builtin_amdgcn_mfma_f32_16x16x32_bf16(WF[t], XF[m], acc[m][t], 0, 0, 0); \
    _Pragma("unroll") for (int t = 0; t < 7; ++t) { \
        __builtin_amdgcn_sched_group_barrier(0x8, 1, 0); \
        __builtin_amdgcn_sched_group_barrier(0x100, 2, 0); } \
    __builtin_amdgcn_sched_group_barrier(0x8, 13, 0); \
    __builtin_amdgcn_sched_barrier(0);

#pragma unroll 1
    for (int c = 0; c < NCH; ++c) {
        const char* wb = smem + (c & (NWB - 1)) * WBUF + trbase;
        const char* wbn = smem + ((c + 1) & (NWB - 1)) * WBUF + trbase;
        const int ab = (c & (NAB - 1)) * ABUF, abn = ((c + 1) & (NAB - 1)) * ABUF;
        __builtin_amdgcn_sched_barrier(0);
        ld_all(wb, ab, 1, wfB, xfB);
        MFMA20(wfA, xfA);
        ld_all(wbn, abn, 0, wfA, xfA);
        MFMA20(wfB, xfB);
        if (c + 1 < NCH) {
            unsigned long long tb = 0, tc = 0;
            asm volatile("s_waitcnt lgkmcnt(0)" ::: "memory");
            TNOW(tb);
            __builtin_amdgcn_s_barrier();
            TNOW(tc);
            bsum += tc - tb;
        }
    }
    STAMP(3);

    {
        constexpr int OP = 176;
        char* ot = smem + wid * (64 * OP);
        asm volatile("s_waitcnt lgkmcnt(0)" ::: "memory");
        __builtin_amdgcn_s_barrier();
#pragma unroll
        for (int m = 0; m < 4; ++m)
#pragma unroll
            for (int t = 0; t < 5; ++t) {
                u32x2 v;
                v[0] = pk_bf16(acc[m][t][0], acc[m][t][1]);
                v[1] = pk_bf16(acc[m][t][2], acc[m][t][3]);
                *reinterpret_cast<u32x2*>(ot + (16 * m + l15) * OP + (16 * t + 4 * lq) * 2) = v;
            }
        char* ob = reinterpret_cast<char*>(out) + (size_t)(tm * BM + wm * 64) * 1280 + (col0 + wn * 80) * 2;
#pragma unroll
        for (int i = 0; i < 10; ++i) {
            const int o = i * 64 + lane;
            const int rr = o / 10, cc = o % 10;
            const u32x4 v = *reinterpret_cast<const u32x4*>(ot + rr * OP + cc * 16);
            *reinterpret_cast<u32x4*>(ob + rr * 1280 + cc * 16) = v;
        }
    }
    STAMP(4);
    STAMP(5);
}

extern "C" void kernel_launch(void* const* d_in, const int* in_sizes, int n_in,
                              void* d_out, int out_size, void* d_ws, size_t ws_size,
                              hipStream_t stream) {
    const u16* x = (const u16*)d_in[0];
    const u16* g = (const u16*)d_in[1];
    const u16* b = (const u16*)d_in[2];
    const u16* w = (const u16*)d_in[3];
    u16* out = (u16*)d_out;
    ln_linear_kernel<<<256, NWAVE * 64, 0, stream>>>(x, g, b, w, out);
}
```

```cpp
#include <hip/hip_runtime.h>
#include <hip/hip_bf16.h>
#include <stdint.h>

typedef __attribute__((ext_vector_type(8))) short bf16x8;
typedef __attribute__((ext_vector_type(4))) short s16x4;
typedef __attribute__((ext_vector_type(4))) float f32x4;
typedef __attribute__((ext_vector_type(4))) unsigned u32x4;
typedef __attribute__((ext_vector_type(2))) unsigned u32x2;
typedef __attribute__((ext_vector_type(2))) __bf16 bf16x2_t;
typedef unsigned short u16;
typedef __attribute__((address_space(3))) char lds_char;

constexpr int D = 640;
constexpr int BM = 128, BN = 160, KC = 64, NCH = D / KC;
constexpr int WPITCH = 352;
constexpr int WBUF = KC * WPITCH;
constexpr int NWB = 4;
constexpr int ABUF = BM * KC * 2;
constexpr int NAB = 4;
constexpr int A_OFF = NWB * WBUF;
constexpr int GB_OFF = A_OFF + NAB * ABUF;
constexpr int SMEM_TOTAL = GB_OFF + 2 * D * 4;
constexpr int NWAVE = 12;

__device__ __forceinline__ float lo_f(unsigned u) { return __builtin_bit_cast(float, u << 16); }
__device__ __forceinline__ float hi_f(unsigned u) { return __builtin_bit_cast(float, u & 0xffff0000u); }
__device__ __forceinline__ unsigned pk_bf16(float a, float b) {
    bf16x2_t v; v[0] = (__bf16)a; v[1] = (__bf16)b;
    return __builtin_bit_cast(unsigned, v);
}
__device__ __forceinline__ float fma1(float a, float b, float c) {
    float r; asm("v_fma_f32 %0, %1, %2, %3" : "=v"(r) : "v"(a), "v"(b), "v"(c)); return r;
}
__device__ __forceinline__ s16x4 tr_read(const char* p) {
    return __builtin_bit_cast(s16x4, __builtin_amdgcn_ds_read_tr16_b64_v4i16(
        (__attribute__((address_space(3))) s16x4*)p));
}
__device__ __forceinline__ unsigned lds_addr(const void* p) {
    return (unsigned)(uintptr_t)(lds_char*)p;
}
__device__ __forceinline__ void glds16(const void* sbase, unsigned voff, unsigned ldst) {
    asm volatile("s_mov_b32 m0, %2\n\ts_nop 0\n\tglobal_load_lds_dwordx4 %0, %1"
                 :: "v"(voff), "s"(sbase), "s"(ldst) : "memory");
}


__global__ __launch_bounds__(NWAVE * 64, 3) void ln_linear_kernel(
    const u16* __restrict__ x, const u16* __restrict__ g, const u16* __restrict__ b,
    const u16* __restrict__ w, u16* __restrict__ out
    )
{
    __shared__ __attribute__((aligned(16))) char smem[SMEM_TOTAL];
    const int tid = threadIdx.x, lane = tid & 63;
    const int wid = __builtin_amdgcn_readfirstlane(tid >> 6);
    const int bid = blockIdx.x;
    const int xcd = bid & 7, idx = bid >> 3;
    const int tn = idx & 3, tm = xcd * 8 + (idx >> 2);
    const int col0 = tn * BN;
    float* gsm = reinterpret_cast<float*>(smem + GB_OFF);
    float* bsm = gsm + D;

    if (wid >= 4) {
        const int hw = wid - 4;
        const int lr = lane >> 3, lc = lane & 7;
        const int rbase = hw * 16;
        u32x4 xv[2][NCH];
        {
            const char* xp = reinterpret_cast<const char*>(x) + (size_t)(tm * BM + rbase + lr) * 1280 + lc * 16;
#pragma unroll
            for (int c = 0; c < NCH; ++c)
#pragma unroll
                for (int i = 0; i < 2; ++i)
                    xv[i][c] = *reinterpret_cast<const u32x4*>(xp + i * 8 * 1280 + c * 128);
        }
        const unsigned smem_base = lds_addr(smem);
        unsigned wvoff[3];
#pragma unroll
        for (int i = 0; i < 3; ++i) {
            int pc = hw + 8 * i;
            int o = pc * 1024 + lane * 16;
            int pos = o / WPITCH, sl = (o % WPITCH) >> 4;
            pos = pos < KC ? pos : 0;
            sl = sl < 20 ? sl : 0;
            int k = (pos & ~12) | ((pos & 4) << 1) | ((pos & 8) >> 1);
            wvoff[i] = k * (D * 2) + sl * 16;
        }
        const char* wbase = reinterpret_cast<const char*>(w) + col0 * 2;
        auto stage_w = [&](int c) {
#pragma unroll
            for (int i = 0; i < 3; ++i)
                if (i < 2 || hw < 6)
                    glds16(wbase + (size_t)c * KC * D * 2, wvoff[i],
                           smem_base + (c & (NWB - 1)) * WBUF + (hw + 8 * i) * 1024);
        };
        stage_w(0); stage_w(1); stage_w(2);
        {
            const int t = tid - 256;
            if (t < 160) {
                const u32x2 gv = *reinterpret_cast<const u32x2*>(g + t * 4);
                const u32x2 bv = *reinterpret_cast<const u32x2*>(b + t * 4);
                f32x4 gf = {lo_f(gv[0]), hi_f(gv[0]), lo_f(gv[1]), hi_f(gv[1])};
                f32x4 bf = {lo_f(bv[0]), hi_f(bv[0]), lo_f(bv[1]), hi_f(bv[1])};
                *reinterpret_cast<f32x4*>(gsm + t * 4) = gf;
                *reinterpret_cast<f32x4*>(bsm + t * 4) = bf;
            }
        }
        float rs[2], nm[2];
        {
            const bf16x2_t ones = __builtin_bit_cast(bf16x2_t, 0x3f803f80u);
            float s1[2][2] = {{0.f, 0.f}, {0.f, 0.f}}, s2[2][2] = {{0.f, 0.f}, {0.f, 0.f}};
#pragma unroll
            for (int c = 0; c < NCH; ++c)
#pragma unroll
                for (int j = 0; j < 4; ++j)
#pragma unroll
                    for (int i = 0; i < 2; ++i) {
                        const unsigned u = xv[i][c][j];
                        bf16x2_t v = __builtin_bit_cast(bf16x2_t, u);
                        s1[i][j & 1] = __builtin_amdgcn_fdot2_f32_bf16(v, ones, s1[i][j & 1], false);
                        s2[i][j & 1] = __builtin_amdgcn_fdot2_f32_bf16(v, v, s2[i][j & 1], false);
                    }
#pragma unroll
            for (int i = 0; i < 2; ++i) {
                float a1 = s1[i][0] + s1[i][1], a2 = s2[i][0] + s2[i][1];
                a1 += __shfl_xor(a1, 1); a2 += __shfl_xor(a2, 1);
                a1 += __shfl_xor(a1, 2); a2 += __shfl_xor(a2, 2);
                a1 += __shfl_xor(a1, 4); a2 += __shfl_xor(a2, 4);
                const float mean = a1 * (1.0f / D);
                float var = a2 * (1.0f / D) - mean * mean;
                var = var < 0.f ? 0.f : var;
                rs[i] = __builtin_amdgcn_rsqf(var + 1e-5f);
                nm[i] = -mean * rs[i];
            }
        }
        asm volatile("s_waitcnt lgkmcnt(0)" ::: "memory");
        __builtin_amdgcn_s_barrier();
        const int awr = A_OFF + (rbase + lr) * 128;
        auto norm_chunk = [&](int c) {
            const f32x4 g0 = *reinterpret_cast<const f32x4*>(gsm + c * 64 + lc * 8);
            const f32x4 g1 = *reinterpret_cast<const f32x4*>(gsm + c * 64 + lc * 8 + 4);
            const f32x4 b0 = *reinterpret_cast<const f32x4*>(bsm + c * 64 + lc * 8);
            const f32x4 b1 = *reinterpret_cast<const f32x4*>(bsm + c * 64 + lc * 8 + 4);
            float f[2][8];
#pragma unroll
            for (int i = 0; i < 2; ++i)
#pragma unroll
                for (int j = 0; j < 4; ++j) {
                    const unsigned u = xv[i][c][j];
                    f[i][2 * j] = lo_f(u); f[i][2 * j + 1] = hi_f(u);
                }
#pragma unroll
            for (int i = 0; i < 2; ++i)
#pragma unroll
                for (int j = 0; j < 8; ++j) f[i][j] = fma1(f[i][j], rs[i], nm[i]);
#pragma unroll
            for (int i = 0; i < 2; ++i)
#pragma unroll
                for (int j = 0; j < 4; ++j) {
                    f[i][j] = fma1(f[i][j], g0[j], b0[j]);
                    f[i][j + 4] = fma1(f[i][j + 4], g1[j], b1[j]);
                }
#pragma unroll
            for (int i = 0; i < 2; ++i) {
                u32x4 o;
#pragma unroll
                for (int j = 0; j < 4; ++j) o[j] = pk_bf16(f[i][2 * j], f[i][2 * j + 1]);
                const int rr = rbase + i * 8 + lr;
                *reinterpret_cast<u32x4*>(smem + awr + (c & (NAB - 1)) * ABUF + i * 1024 + ((lc ^ ((rr >> 1) & 7)) << 4)) = o;
            }
        };
        norm_chunk(0);
        norm_chunk(1);
#pragma unroll
        for (int c = 0; c < NCH; ++c) {
            if (c + 2 < NCH) {
                if (hw < 6) asm volatile("s_waitcnt vmcnt(3)" ::: "memory");
                else        asm volatile("s_waitcnt vmcnt(2)" ::: "memory");
            } else          asm volatile("s_waitcnt vmcnt(0)" ::: "memory");
            asm volatile("s_waitcnt lgkmcnt(0)" ::: "memory");
            __builtin_amdgcn_s_barrier();
            if (c + 3 < NCH) stage_w(c + 3);
            if (c + 2 < NCH) norm_chunk(c + 2);
        }
        return;
    }

    const int wm = wid >> 1, wn = wid & 1;
    const int l15 = lane & 15, lq = lane >> 4;
    __builtin_amdgcn_s_barrier();

    f32x4 acc[4][5];
#pragma unroll
    for (int m = 0; m < 4; ++m)
#pragma unroll
        for (int t = 0; t < 5; ++t) acc[m][t] = (f32x4){0.f, 0.f, 0.f, 0.f};

    const int q4 = l15 >> 2, p4 = l15 & 3;
    const int trbase = (16 * (lq >> 1) + 4 * (lq & 1) + q4) * WPITCH + (wn * 80 + 4 * p4) * 2;
    unsigned ard[2];
#pragma unroll
    for (int ks = 0; ks < 2; ++ks)
        ard[ks] = A_OFF + (wm * 64 + l15) * 128 + (((4 * ks + lq) ^ ((l15 >> 1) & 7)) << 4);

    auto ld_w = [&](const char* wb, int ks, bf16x8 (&wf)[5]) {
#pragma unroll
        for (int t = 0; t < 5; ++t) {
            s16x4 w0 = tr_read(wb + ks * 32 * WPITCH + t * 32);
            s16x4 w1 = tr_read(wb + ks * 32 * WPITCH + t * 32 + 8 * WPITCH);
            wf[t] = __builtin_shufflevector(w0, w1, 0, 1, 2, 3, 4, 5, 6, 7);
        }
    };
    auto ld_a = [&](int ab, int ks, bf16x8 (&xf)[4]) {
#pragma unroll
        for (int m = 0; m < 4; ++m)
            xf[m] = *reinterpret_cast<const bf16x8*>(smem + ab + ard[ks] + m * 2048);
    };

    auto ld_all = [&](const char* wb, int ab, int ks, bf16x8 (&wf)[5], bf16x8 (&xf)[4]) {
        xf[0] = *reinterpret_cast<const bf16x8*>(smem + ab + ard[ks]);
        ld_w(wb, ks, wf);
#pragma unroll
        for (int m = 1; m < 4; ++m)
            xf[m] = *reinterpret_cast<const bf16x8*>(smem + ab + ard[ks] + m * 2048);
    };
    bf16x8 wfA[5], wfB[5], xfA[4], xfB[4];
    __builtin_amdgcn_s_barrier();
    __builtin_amdgcn_s_setprio(1);
    ld_a(0, 0, xfA);
    ld_w(smem + trbase, 0, wfA);

#define MFMA20(WF, XF) \
    _Pragma("unroll") for (int m = 0; m < 4; ++m) \
    _Pragma("unroll") for (int t = 0; t < 5; ++t) \
        acc[m][t] = __builtin_amdgcn_mfma_f32_16x16x32_bf16(WF[t], XF[m], acc[m][t], 0, 0, 0); \
    _Pragma("unroll") for (int t = 0; t < 7; ++t) { \
        __builtin_amdgcn_sched_group_barrier(0x8, 1, 0); \
        __builtin_amdgcn_sched_group_barrier(0x100, 2, 0); } \
    __builtin_amdgcn_sched_group_barrier(0x8, 13, 0); \
    __builtin_amdgcn_sched_barrier(0);

#pragma unroll 1
    for (int c = 0; c < NCH; ++c) {
        const char* wb = smem + (c & (NWB - 1)) * WBUF + trbase;
        const char* wbn = smem + ((c + 1) & (NWB - 1)) * WBUF + trbase;
        const int ab = (c & (NAB - 1)) * ABUF, abn = ((c + 1) & (NAB - 1)) * ABUF;
        __builtin_amdgcn_sched_barrier(0);
        ld_all(wb, ab, 1, wfB, xfB);
        MFMA20(wfA, xfA);
        ld_all(wbn, abn, 0, wfA, xfA);
        MFMA20(wfB, xfB);
        if (c + 1 < NCH) {
            asm volatile("s_waitcnt lgkmcnt(0)" ::: "memory");
            __builtin_amdgcn_s_barrier();
        }
    }

    {
        constexpr int OP = 176;
        char* ot = smem + wid * (64 * OP);
        asm volatile("s_waitcnt lgkmcnt(0)" ::: "memory");
        __builtin_amdgcn_s_barrier();
#pragma unroll
        for (int m = 0; m < 4; ++m)
#pragma unroll
            for (int t = 0; t < 5; ++t) {
                u32x2 v;
                v[0] = pk_bf16(acc[m][t][0], acc[m][t][1]);
                v[1] = pk_bf16(acc[m][t][2], acc[m][t][3]);
                *reinterpret_cast<u32x2*>(ot + (16 * m + l15) * OP + (16 * t + 4 * lq) * 2) = v;
            }
        char* ob = reinterpret_cast<char*>(out) + (size_t)(tm * BM + wm * 64) * 1280 + (col0 + wn * 80) * 2;
#pragma unroll
        for (int i = 0; i < 10; ++i) {
            const int o = i * 64 + lane;
            const int rr = o / 10, cc = o % 10;
            const u32x4 v = *reinterpret_cast<const u32x4*>(ot + rr * OP + cc * 16);
            *reinterpret_cast<u32x4*>(ob + rr * 1280 + cc * 16) = v;
        }
    }
}

extern "C" void kernel_launch(void* const* d_in, const int* in_sizes, int n_in,
                              void* d_out, int out_size, void* d_ws, size_t ws_size,
                              hipStream_t stream) {
    const u16* x = (const u16*)d_in[0];
    const u16* g = (const u16*)d_in[1];
    const u16* b = (const u16*)d_in[2];
    const u16* w = (const u16*)d_in[3];
    u16* out = (u16*)d_out;
    ln_linear_kernel<<<256, NWAVE * 64, 0, stream>>>(x, g, b, w, out);
}
```
